```python
import torch

def _softmax(x, axis=-1):
    return torch.softmax(x, dim=axis)

def _gelu(x):
    return torch.nn.functional.gelu(x, approximate='tanh')

def _silu(x):
    return torch.nn.functional.silu(x)

def _sigmoid(x):
    return torch.sigmoid(x)

def _layernorm(x, axis=-1, eps=1e-05):
    m = x.mean(dim=axis, keepdim=True)
    v = x.var(dim=axis, keepdim=True, unbiased=False)
    return (x - m) / torch.sqrt(v + eps)

def _scatter_add(tgt, idx, src, axis=0):
    return tgt.clone().index_add_(axis, idx, src)

def _segment_sum(src, seg, n):
    out = torch.zeros((n,) + src.shape[1:], dtype=src.dtype)
    return out.index_add_(0, seg, src)

def _pad(x, pw):
    flat = tuple((v for pair in reversed(pw) for v in pair))
    return torch.nn.functional.pad(x, flat)
kernel_name = 'standalone__autoregressive_conv_wavenet__speech'

def setup_inputs():

    _w = lambda a: (lambda t: t.to(torch.bfloat16) if t.is_floating_point() else t)(torch.as_tensor(a))
    B, T, D, K = (2, 4000, 256, 3)
    return {'x': _w((lambda *s: torch.randn(*s, dtype=torch.bfloat16) * 0.1)(B, T, D)), 'w': _w((lambda *s: torch.randn(*s, dtype=torch.bfloat16) * 0.1)(K, D, 2 * D)), 'w_o': _w((lambda *s: torch.randn(*s, dtype=torch.bfloat16) * 0.05)(D, D))}

def reference(x, w, w_o):
    B, T, D = x.shape
    K = w.shape[0]
    dil = 2
    pad = (K - 1) * dil
    xp = _pad(x, ((0, 0), (pad, 0), (0, 0)))
    h = torch.einsum('btc,cd->btd', xp[:, pad:pad + T, :], w[0])
    for i in range(1, K):
        off = pad - i * dil
        h = h + torch.einsum('btc,cd->btd', xp[:, off:off + T, :], w[i])
    f = h[..., :D].clone()
    g = h[..., D:].clone()
    z = torch.tanh(f) * _sigmoid(g)
    return z @ w_o
```

```cpp
#include <hip/hip_runtime.h>
#include <hip/hip_bf16.h>
#include <stdint.h>

#define TT 4000
#define DD 256

typedef __attribute__((ext_vector_type(8))) short bf16x8;
typedef __attribute__((ext_vector_type(4))) short s16x4;
typedef __attribute__((ext_vector_type(16))) float f32x16;
typedef __attribute__((ext_vector_type(4))) unsigned u32x4;
typedef __attribute__((ext_vector_type(2))) unsigned u32x2;
typedef __attribute__((address_space(3))) s16x4 lds_s16x4;

#define MFMA(a, b, c) __builtin_amdgcn_mfma_f32_32x32x16_bf16((a), (b), (c), 0, 0, 0)

__device__ __forceinline__ s16x4 trread(const unsigned char* p) {
    return __builtin_amdgcn_ds_read_tr16_b64_v4i16((lds_s16x4*)p);
}
__device__ __forceinline__ bf16x8 cat8(s16x4 a, s16x4 b) {
    bf16x8 r;
    r[0] = a[0]; r[1] = a[1]; r[2] = a[2]; r[3] = a[3];
    r[4] = b[0]; r[5] = b[1]; r[6] = b[2]; r[7] = b[3];
    return r;
}
__device__ __forceinline__ unsigned pack2(float a, float b) {
    __hip_bfloat16 x = __float2bfloat16(a), y = __float2bfloat16(b);
    return (unsigned)__builtin_bit_cast(unsigned short, x) | ((unsigned)__builtin_bit_cast(unsigned short, y) << 16);
}
__device__ __forceinline__ float gate(float f, float g) {
    float e2 = __expf(2.0f * f);
    float th = 1.0f - 2.0f / (e2 + 1.0f);
    float sg = 1.0f / (1.0f + __expf(-g));
    return th * sg;
}

#define XS_STRIDE 528
#define XS_BYTES (132 * XS_STRIDE)
#define WS_STRIDE 320
#define WS_BYTES (64 * WS_STRIDE)

__global__ __launch_bounds__(256) void k1_conv_gate(const unsigned short* __restrict__ x,
                                                   const unsigned short* __restrict__ w,
                                                   unsigned short* __restrict__ z) {
    __shared__ __attribute__((aligned(16))) unsigned char smem[XS_BYTES + 3 * WS_BYTES];
    unsigned char* xs = smem;
    unsigned char* wsb = smem + XS_BYTES;

    const int bid = blockIdx.x;
    const int xcd = bid & 7;
    const int nq = (bid >> 3) & 3;
    const int mhi = bid >> 5;
    const int mt = xcd + 8 * mhi;
    const int b = mt >> 5;
    const int t0 = (mt & 31) * 128;
    const int tid = threadIdx.x;
    const int lane = tid & 63;
    const int wv = tid >> 6;

    const unsigned short* wsrc[4];
    int wdst[4];
#pragma unroll
    for (int i = 0; i < 4; ++i) {
        int p = tid + 256 * i;
        int row = p >> 4, c16 = p & 15;
        int col = (c16 < 8) ? (nq * 64 + c16 * 8) : (256 + nq * 64 + (c16 - 8) * 8);
        wsrc[i] = w + (size_t)row * 512 + col;
        wdst[i] = row * WS_STRIDE + c16 * 16;
    }
    u32x4 wreg[4];
#pragma unroll
    for (int i = 0; i < 4; ++i) wreg[i] = *(const u32x4*)(wsrc[i]);

    {
        u32x4 xr[17];
#pragma unroll
        for (int i = 0; i < 17; ++i) {
            int p = tid + 256 * i;
            int row = p >> 5, c16 = p & 31;
            int t = t0 - 4 + row;
            u32x4 v = {0u, 0u, 0u, 0u};
            if (p < 4224 && t >= 0 && t < TT) v = *(const u32x4*)(x + ((size_t)b * TT + t) * DD + c16 * 8);
            xr[i] = v;
        }
#pragma unroll
        for (int i = 0; i < 17; ++i) {
            int p = tid + 256 * i;
            int row = p >> 5, c16 = p & 31;
            if (p < 4224) *(u32x4*)(xs + row * XS_STRIDE + c16 * 16) = xr[i];
        }
    }
#pragma unroll
    for (int i = 0; i < 4; ++i) *(u32x4*)(wsb + wdst[i]) = wreg[i];
#pragma unroll
    for (int i = 0; i < 4; ++i) wreg[i] = *(const u32x4*)(wsrc[i] + 64 * 512);
    __syncthreads();

    const int fi = wv & 1;
    const int jt = wv >> 1;
    const int r = lane & 31, hh = lane >> 5;
    const int grp = lane >> 4, q = (lane & 15) >> 2, pp = lane & 3;
    const int a_off_f = (8 * hh + q) * WS_STRIDE + (32 * fi + 16 * (grp & 1) + 4 * pp) * 2;
    const int a_off_g = a_off_f + 128;
    const int b_off = (jt * 64 + r + 4) * XS_STRIDE + hh * 16;

    f32x16 accF[2], accG[2];
#pragma unroll
    for (int i = 0; i < 16; ++i) { accF[0][i] = 0.f; accF[1][i] = 0.f; accG[0][i] = 0.f; accG[1][i] = 0.f; }

    for (int ch = 0; ch < 12; ++ch) {
        const unsigned char* wb = wsb + (ch % 3) * WS_BYTES;
        if (ch + 1 < 12) {
            unsigned char* wn = wsb + ((ch + 1) % 3) * WS_BYTES;
#pragma unroll
            for (int i = 0; i < 4; ++i) *(u32x4*)(wn + wdst[i]) = wreg[i];
            if (ch + 2 < 12) {
#pragma unroll
                for (int i = 0; i < 4; ++i) wreg[i] = *(const u32x4*)(wsrc[i] + (size_t)(ch + 2) * 64 * 512);
            }
        }
        const int tap = ch >> 2;
        const int c0 = (ch & 3) * 64;
        const unsigned char* xb = xs + b_off - 2 * tap * XS_STRIDE + c0 * 2;
#pragma unroll
        for (int ks = 0; ks < 4; ++ks) {
            s16x4 f0 = trread(wb + a_off_f + ks * 16 * WS_STRIDE);
            s16x4 f1 = trread(wb + a_off_f + ks * 16 * WS_STRIDE + 4 * WS_STRIDE);
            s16x4 g0 = trread(wb + a_off_g + ks * 16 * WS_STRIDE);
            s16x4 g1 = trread(wb + a_off_g + ks * 16 * WS_STRIDE + 4 * WS_STRIDE);
            bf16x8 bx0 = *(const bf16x8*)(xb + ks * 32);
            bf16x8 bx1 = *(const bf16x8*)(xb + ks * 32 + 32 * XS_STRIDE);
            bf16x8 af = cat8(f0, f1), ag = cat8(g0, g1);
            accF[0] = MFMA(af, bx0, accF[0]);
            accG[0] = MFMA(ag, bx0, accG[0]);
            accF[1] = MFMA(af, bx1, accF[1]);
            accG[1] = MFMA(ag, bx1, accG[1]);
        }
        __syncthreads();
    }

#pragma unroll
    for (int tt = 0; tt < 2; ++tt) {
        int t = t0 + jt * 64 + tt * 32 + r;
        if (t < TT) {
            unsigned short* zr = z + ((size_t)b * TT + t) * DD + nq * 64 + 32 * fi + 4 * hh;
#pragma unroll
            for (int g4 = 0; g4 < 4; ++g4) {
                u32x2 v;
                v[0] = pack2(gate(accF[tt][4 * g4 + 0], accG[tt][4 * g4 + 0]), gate(accF[tt][4 * g4 + 1], accG[tt][4 * g4 + 1]));
                v[1] = pack2(gate(accF[tt][4 * g4 + 2], accG[tt][4 * g4 + 2]), gate(accF[tt][4 * g4 + 3], accG[tt][4 * g4 + 3]));
                *(u32x2*)(zr + 8 * g4) = v;
            }
        }
    }
}

#define WO_STRIDE 320
__global__ __launch_bounds__(256) void k2_out(const unsigned short* __restrict__ z,
                                             const unsigned short* __restrict__ wo,
                                             unsigned short* __restrict__ out) {
    __shared__ __attribute__((aligned(16))) unsigned char smem[256 * WO_STRIDE];
    const int bid = blockIdx.x;
    const int oh = bid & 1;
    const int mt = bid >> 1;
    const int tid = threadIdx.x, lane = tid & 63, wv = tid >> 6;
    const int r = lane & 31, hh = lane >> 5;
    const int ti = wv & 1, oi = wv >> 1;

    const unsigned short* zr = z + (size_t)(mt * 64 + ti * 32 + r) * DD + hh * 8;
    bf16x8 zf[16];
#pragma unroll
    for (int ks = 0; ks < 16; ++ks) zf[ks] = *(const bf16x8*)(zr + ks * 16);

    {
        u32x4 v[16];
#pragma unroll
        for (int i = 0; i < 16; ++i) {
            int p = tid + 256 * i;
            v[i] = *(const u32x4*)(wo + (size_t)(p >> 4) * DD + oh * 128 + (p & 15) * 8);
        }
#pragma unroll
        for (int i = 0; i < 16; ++i) {
            int p = tid + 256 * i;
            *(u32x4*)(smem + (p >> 4) * WO_STRIDE + (p & 15) * 16) = v[i];
        }
    }
    __syncthreads();

    const int grp = lane >> 4, q = (lane & 15) >> 2, pp = lane & 3;
    const unsigned char* ab = smem + (8 * hh + q) * WO_STRIDE + (oi * 64 + 16 * (grp & 1) + 4 * pp) * 2;
    f32x16 acc[2];
#pragma unroll
    for (int i = 0; i < 16; ++i) { acc[0][i] = 0.f; acc[1][i] = 0.f; }
#pragma unroll
    for (int ks = 0; ks < 16; ++ks) {
        s16x4 a0 = trread(ab + ks * 16 * WO_STRIDE);
        s16x4 a1 = trread(ab + ks * 16 * WO_STRIDE + 4 * WO_STRIDE);
        s16x4 c0 = trread(ab + ks * 16 * WO_STRIDE + 64);
        s16x4 c1 = trread(ab + ks * 16 * WO_STRIDE + 4 * WO_STRIDE + 64);
        acc[0] = MFMA(cat8(a0, a1), zf[ks], acc[0]);
        acc[1] = MFMA(cat8(c0, c1), zf[ks], acc[1]);
    }
    unsigned short* orow = out + (size_t)(mt * 64 + ti * 32 + r) * DD + oh * 128 + oi * 64 + 4 * hh;
#pragma unroll
    for (int ot = 0; ot < 2; ++ot) {
#pragma unroll
        for (int g4 = 0; g4 < 4; ++g4) {
            u32x2 v;
            v[0] = pack2(acc[ot][4 * g4 + 0], acc[ot][4 * g4 + 1]);
            v[1] = pack2(acc[ot][4 * g4 + 2], acc[ot][4 * g4 + 3]);
            *(u32x2*)(orow + ot * 32 + 8 * g4) = v;
        }
    }
}

extern "C" void kernel_launch(void* const* d_in, const int* in_sizes, int n_in,
                              void* d_out, int out_size, void* d_ws, size_t ws_size,
                              hipStream_t stream) {
    const unsigned short* x = (const unsigned short*)d_in[0];
    const unsigned short* w = (const unsigned short*)d_in[1];
    const unsigned short* wo = (const unsigned short*)d_in[2];
    unsigned short* out = (unsigned short*)d_out;
    unsigned short* z = (unsigned short*)d_ws;
    k1_conv_gate<<<256, 256, 0, stream>>>(x, w, z);
    k2_out<<<250, 256, 0, stream>>>(z, wo, out);
}
```

```cpp
#include <hip/hip_runtime.h>
#include <hip/hip_bf16.h>
#include <stdint.h>

#define TT 4000
#define DD 256

typedef __attribute__((ext_vector_type(8))) short bf16x8;
typedef __attribute__((ext_vector_type(4))) short s16x4;
typedef __attribute__((ext_vector_type(16))) float f32x16;
typedef __attribute__((ext_vector_type(4))) unsigned u32x4;
typedef __attribute__((ext_vector_type(2))) unsigned u32x2;
typedef __attribute__((address_space(3))) s16x4 lds_s16x4;

#define MFMA(a, b, c) __builtin_amdgcn_mfma_f32_32x32x16_bf16((a), (b), (c), 0, 0, 0)

__device__ __forceinline__ s16x4 trread(const unsigned char* p) {
    return __builtin_amdgcn_ds_read_tr16_b64_v4i16((lds_s16x4*)p);
}
__device__ __forceinline__ bf16x8 cat8(s16x4 a, s16x4 b) {
    bf16x8 r;
    r[0] = a[0]; r[1] = a[1]; r[2] = a[2]; r[3] = a[3];
    r[4] = b[0]; r[5] = b[1]; r[6] = b[2]; r[7] = b[3];
    return r;
}
__device__ __forceinline__ unsigned pack2(float a, float b) {
    __hip_bfloat16 x = __float2bfloat16(a), y = __float2bfloat16(b);
    return (unsigned)__builtin_bit_cast(unsigned short, x) | ((unsigned)__builtin_bit_cast(unsigned short, y) << 16);
}
__device__ __forceinline__ float gate(float f, float g) {
    float e2 = __expf(2.0f * f);
    float th = 1.0f - 2.0f / (e2 + 1.0f);
    float sg = 1.0f / (1.0f + __expf(-g));
    return th * sg;
}

#define XS_BYTES (132 * 512)
#define WCH_BYTES (64 * 256)
#define NRING 5
#define RAW_BARRIER() do { asm volatile("s_waitcnt lgkmcnt(0)" ::: "memory"); __builtin_amdgcn_s_barrier(); } while (0)
#define GLDS(src, dst) __builtin_amdgcn_global_load_lds((const unsigned*)(src), (unsigned*)(dst), 16, 0, 0)

__device__ __forceinline__ int wswz(int row) { return ((row & 3) << 2) | ((row >> 2) & 3); }

__global__ __launch_bounds__(256) void k1_conv_gate(const unsigned short* __restrict__ x,
                                                   const unsigned short* __restrict__ w,
                                                   const unsigned short* __restrict__ zero16,
                                                   unsigned short* __restrict__ z) {
    __shared__ __attribute__((aligned(16))) unsigned char smem[XS_BYTES + NRING * WCH_BYTES];
    unsigned char* xs = smem;
    unsigned char* wsb = smem + XS_BYTES;

    const int bid = blockIdx.x;
    const int xcd = bid & 7;
    const int nq = (bid >> 3) & 3;
    const int mhi = bid >> 5;
    const int mt = xcd + 8 * mhi;
    const int b = mt >> 5;
    const int t0 = (mt & 31) * 128;
    const int tid = threadIdx.x;
    const int lane = tid & 63;
    const int wv = tid >> 6;

    int woff[4];
#pragma unroll
    for (int i = 0; i < 4; ++i) {
        int pos = (wv * 4 + i) * 64 + lane;
        int row = pos >> 4, ch = (pos & 15) ^ wswz(row);
        int col = (ch < 8) ? (nq * 64 + ch * 8) : (256 + nq * 64 + (ch - 8) * 8);
        woff[i] = row * 512 + col;
    }
#pragma unroll
    for (int i = 0; i < 4; ++i) GLDS(w + woff[i], wsb + (wv * 4 + i) * 1024);
#pragma unroll
    for (int i = 0; i < 17; ++i) {
        int ins = wv + 4 * i;
        if (ins < 66) {
            int row = ins * 2 + (lane >> 5);
            int c16 = (lane & 31) ^ (row & 15);
            int t = t0 - 4 + row;
            const unsigned short* src = (t >= 0 && t < TT) ? (x + ((size_t)b * TT + t) * DD + c16 * 8) : zero16;
            GLDS(src, xs + ins * 1024);
        }
    }
#pragma unroll
    for (int c = 1; c < 4; ++c)
#pragma unroll
        for (int i = 0; i < 4; ++i) GLDS(w + woff[i] + c * 64 * 512, wsb + c * WCH_BYTES + (wv * 4 + i) * 1024);

    const int fi = wv & 1;
    const int jt = wv >> 1;
    const int r = lane & 31, hh = lane >> 5;
    const int grp = lane >> 4, q = (lane & 15) >> 2, pp = lane & 3;
    const int arow0 = 8 * hh + q, arow1 = arow0 + 4;
    const int ach = 4 * fi + 2 * (grp & 1) + (pp >> 1);
    const int af0 = arow0 * 256 + ((ach ^ wswz(arow0)) << 4) + 8 * (pp & 1);
    const int af1 = arow1 * 256 + ((ach ^ wswz(arow1)) << 4) + 8 * (pp & 1);
    const int ag0 = arow0 * 256 + (((ach + 8) ^ wswz(arow0)) << 4) + 8 * (pp & 1);
    const int ag1 = arow1 * 256 + (((ach + 8) ^ wswz(arow1)) << 4) + 8 * (pp & 1);

    f32x16 accF[2], accG[2];
#pragma unroll
    for (int i = 0; i < 16; ++i) { accF[0][i] = 0.f; accF[1][i] = 0.f; accG[0][i] = 0.f; accG[1][i] = 0.f; }

#pragma unroll 1
    for (int ch = 0; ch < 12; ++ch) {
        if (ch < 9) asm volatile("s_waitcnt vmcnt(12)" ::: "memory");
        else if (ch == 9) asm volatile("s_waitcnt vmcnt(8)" ::: "memory");
        else if (ch == 10) asm volatile("s_waitcnt vmcnt(4)" ::: "memory");
        else asm volatile("s_waitcnt vmcnt(0)" ::: "memory");
        RAW_BARRIER();
        if (ch + 4 < 12) {
            unsigned char* wn = wsb + ((ch + 4) % NRING) * WCH_BYTES;
#pragma unroll
            for (int i = 0; i < 4; ++i) GLDS(w + woff[i] + (ch + 4) * 64 * 512, wn + (wv * 4 + i) * 1024);
        }
        const unsigned char* wb = wsb + (ch % NRING) * WCH_BYTES;
        const int tap = ch >> 2;
        const int xrow = jt * 64 + r + 4 - 2 * tap;
        const unsigned char* xb = xs + xrow * 512;
        const int xsw = xrow & 15;
        const int cb = (ch & 3) * 8 + hh;
#pragma unroll
        for (int ks = 0; ks < 4; ++ks) {
            s16x4 f0 = trread(wb + af0 + ks * 4096);
            s16x4 f1 = trread(wb + af1 + ks * 4096);
            s16x4 g0 = trread(wb + ag0 + ks * 4096);
            s16x4 g1 = trread(wb + ag1 + ks * 4096);
            const int xo = ((cb + 2 * ks) ^ xsw) << 4;
            bf16x8 bx0 = *(const bf16x8*)(xb + xo);
            bf16x8 bx1 = *(const bf16x8*)(xb + xo + 32 * 512);
            bf16x8 af = cat8(f0, f1), ag = cat8(g0, g1);
            accF[0] = MFMA(af, bx0, accF[0]);
            accG[0] = MFMA(ag, bx0, accG[0]);
            accF[1] = MFMA(af, bx1, accF[1]);
            accG[1] = MFMA(ag, bx1, accG[1]);
        }
    }

#pragma unroll
    for (int tt = 0; tt < 2; ++tt) {
        int t = t0 + jt * 64 + tt * 32 + r;
        if (t < TT) {
            unsigned short* zr = z + ((size_t)b * TT + t) * DD + nq * 64 + 32 * fi + 4 * hh;
#pragma unroll
            for (int g4 = 0; g4 < 4; ++g4) {
                u32x2 v;
                v[0] = pack2(gate(accF[tt][4 * g4 + 0], accG[tt][4 * g4 + 0]), gate(accF[tt][4 * g4 + 1], accG[tt][4 * g4 + 1]));
                v[1] = pack2(gate(accF[tt][4 * g4 + 2], accG[tt][4 * g4 + 2]), gate(accF[tt][4 * g4 + 3], accG[tt][4 * g4 + 3]));
                *(u32x2*)(zr + 8 * g4) = v;
            }
        }
    }
}

#define WO_STRIDE 320
__global__ __launch_bounds__(256) void k2_out(const unsigned short* __restrict__ z,
                                             const unsigned short* __restrict__ wo,
                                             unsigned short* __restrict__ out) {
    __shared__ __attribute__((aligned(16))) unsigned char smem[256 * WO_STRIDE];
    const int bid = blockIdx.x;
    const int oh = bid & 1;
    const int mt = bid >> 1;
    const int tid = threadIdx.x, lane = tid & 63, wv = tid >> 6;
    const int r = lane & 31, hh = lane >> 5;
    const int ti = wv & 1, oi = wv >> 1;

    const unsigned short* zr = z + (size_t)(mt * 64 + ti * 32 + r) * DD + hh * 8;
    bf16x8 zf[16];
#pragma unroll
    for (int ks = 0; ks < 16; ++ks) zf[ks] = *(const bf16x8*)(zr + ks * 16);

    {
        u32x4 v[16];
#pragma unroll
        for (int i = 0; i < 16; ++i) {
            int p = tid + 256 * i;
            v[i] = *(const u32x4*)(wo + (size_t)(p >> 4) * DD + oh * 128 + (p & 15) * 8);
        }
#pragma unroll
        for (int i = 0; i < 16; ++i) {
            int p = tid + 256 * i;
            *(u32x4*)(smem + (p >> 4) * WO_STRIDE + (p & 15) * 16) = v[i];
        }
    }
    __syncthreads();

    const int grp = lane >> 4, q = (lane & 15) >> 2, pp = lane & 3;
    const unsigned char* ab = smem + (8 * hh + q) * WO_STRIDE + (oi * 64 + 16 * (grp & 1) + 4 * pp) * 2;
    f32x16 acc[2];
#pragma unroll
    for (int i = 0; i < 16; ++i) { acc[0][i] = 0.f; acc[1][i] = 0.f; }
#pragma unroll
    for (int ks = 0; ks < 16; ++ks) {
        s16x4 a0 = trread(ab + ks * 16 * WO_STRIDE);
        s16x4 a1 = trread(ab + ks * 16 * WO_STRIDE + 4 * WO_STRIDE);
        s16x4 c0 = trread(ab + ks * 16 * WO_STRIDE + 64);
        s16x4 c1 = trread(ab + ks * 16 * WO_STRIDE + 4 * WO_STRIDE + 64);
        acc[0] = MFMA(cat8(a0, a1), zf[ks], acc[0]);
        acc[1] = MFMA(cat8(c0, c1), zf[ks], acc[1]);
    }
    unsigned short* orow = out + (size_t)(mt * 64 + ti * 32 + r) * DD + oh * 128 + oi * 64 + 4 * hh;
#pragma unroll
    for (int ot = 0; ot < 2; ++ot) {
#pragma unroll
        for (int g4 = 0; g4 < 4; ++g4) {
            u32x2 v;
            v[0] = pack2(acc[ot][4 * g4 + 0], acc[ot][4 * g4 + 1]);
            v[1] = pack2(acc[ot][4 * g4 + 2], acc[ot][4 * g4 + 3]);
            *(u32x2*)(orow + ot * 32 + 8 * g4) = v;
        }
    }
}

extern "C" void kernel_launch(void* const* d_in, const int* in_sizes, int n_in,
                              void* d_out, int out_size, void* d_ws, size_t ws_size,
                              hipStream_t stream) {
    const unsigned short* x = (const unsigned short*)d_in[0];
    const unsigned short* w = (const unsigned short*)d_in[1];
    const unsigned short* wo = (const unsigned short*)d_in[2];
    unsigned short* out = (unsigned short*)d_out;
    unsigned short* z = (unsigned short*)d_ws;
    unsigned short* zero16 = (unsigned short*)((char*)d_ws + (8 << 20));
    hipMemsetAsync(zero16, 0, 64, stream);
    k1_conv_gate<<<256, 256, 0, stream>>>(x, w, zero16, z);
    k2_out<<<250, 256, 0, stream>>>(z, wo, out);
}
```

```cpp
#include <hip/hip_runtime.h>
#include <hip/hip_bf16.h>
#include <stdint.h>

#define TT 4000
#define DD 256

typedef __attribute__((ext_vector_type(8))) short bf16x8;
typedef __attribute__((ext_vector_type(4))) short s16x4;
typedef __attribute__((ext_vector_type(16))) float f32x16;
typedef __attribute__((ext_vector_type(4))) unsigned u32x4;
typedef __attribute__((ext_vector_type(2))) unsigned u32x2;
typedef __attribute__((address_space(3))) s16x4 lds_s16x4;

#define MFMA(a, b, c) __builtin_amdgcn_mfma_f32_32x32x16_bf16((a), (b), (c), 0, 0, 0)

__device__ __forceinline__ s16x4 trread(const unsigned char* p) {
    return __builtin_amdgcn_ds_read_tr16_b64_v4i16((lds_s16x4*)p);
}
__device__ __forceinline__ bf16x8 cat8(s16x4 a, s16x4 b) {
    bf16x8 r;
    r[0] = a[0]; r[1] = a[1]; r[2] = a[2]; r[3] = a[3];
    r[4] = b[0]; r[5] = b[1]; r[6] = b[2]; r[7] = b[3];
    return r;
}
__device__ __forceinline__ unsigned pack2(float a, float b) {
    __hip_bfloat16 x = __float2bfloat16(a), y = __float2bfloat16(b);
    return (unsigned)__builtin_bit_cast(unsigned short, x) | ((unsigned)__builtin_bit_cast(unsigned short, y) << 16);
}
__device__ __forceinline__ float gate(float f, float g) {
    float e2 = __expf(2.0f * f);
    float th = 1.0f - 2.0f * __builtin_amdgcn_rcpf(e2 + 1.0f);
    float sg = __builtin_amdgcn_rcpf(1.0f + __expf(-g));
    return th * sg;
}

#define XS_BYTES (132 * 512)
#define WCH_BYTES (64 * 256)
#define NRING 5
#define RAW_BARRIER() do { asm volatile("s_waitcnt lgkmcnt(0)" ::: "memory"); __builtin_amdgcn_s_barrier(); } while (0)
__device__ __forceinline__ void glds16(const void* src, const unsigned char* dst) {
    unsigned l = __builtin_amdgcn_readfirstlane((unsigned)(uintptr_t)(__attribute__((address_space(3))) const unsigned char*)dst);
    asm volatile("s_mov_b32 m0, %1\n\ts_nop 0\n\tglobal_load_lds_dwordx4 %0, off" :: "v"(src), "s"(l) : "memory", "m0");
}
#define GLDS(src, dst) glds16((src), (dst))

__device__ __forceinline__ int wswz(int row) { return ((row & 3) << 2) | ((row >> 2) & 3); }

__global__ __launch_bounds__(256) void k1_conv_gate(const unsigned short* __restrict__ x,
                                                   const unsigned short* __restrict__ w,
                                                   const unsigned short* __restrict__ wo,
                                                   unsigned short* __restrict__ z,
                                                   unsigned long long* __restrict__ flags,
                                                   unsigned short* __restrict__ out) {
    __shared__ __attribute__((aligned(16))) unsigned char smem[XS_BYTES + NRING * WCH_BYTES];
    unsigned char* xs = smem;
    unsigned char* wsb = smem + XS_BYTES;

    const int bid = blockIdx.x;
    const int xcd = bid & 7;
    const int nq = (bid >> 3) & 3;
    const int mhi = bid >> 5;
    const int mt = xcd + 8 * mhi;
    const int b = mt >> 5;
    const int t0 = (mt & 31) * 128;
    const int tid = threadIdx.x;
    const int lane = tid & 63;
    const int wv = tid >> 6;

    int woff[4];
#pragma unroll
    for (int i = 0; i < 4; ++i) {
        int pos = (wv * 4 + i) * 64 + lane;
        int row = pos >> 4, ch = (pos & 15) ^ wswz(row);
        int col = (ch < 8) ? (nq * 64 + ch * 8) : (256 + nq * 64 + (ch - 8) * 8);
        woff[i] = row * 512 + col;
    }
    const int rot = (mt * 5) % 12;
#define CHE(c) (((c) + rot) % 12)
#pragma unroll
    for (int i = 0; i < 4; ++i) GLDS(w + woff[i] + CHE(0) * 64 * 512, wsb + (wv * 4 + i) * 1024);
#pragma unroll
    for (int i = 0; i < 17; ++i) {
        int ins = wv + 4 * i;
        if (ins < 66) {
            int row = ins * 2 + (lane >> 5);
            int c16 = (lane & 31) ^ (row & 15);
            int t = t0 - 4 + row;
            int tc = t < 0 ? 0 : (t >= TT ? TT - 1 : t);
            const unsigned short* src = x + ((size_t)b * TT + tc) * DD + c16 * 8;
            GLDS(src, xs + ins * 1024);
        }
    }
#pragma unroll
    for (int c = 1; c < 4; ++c)
#pragma unroll
        for (int i = 0; i < 4; ++i) GLDS(w + woff[i] + CHE(c) * 64 * 512, wsb + c * WCH_BYTES + (wv * 4 + i) * 1024);

    const int fi = wv & 1;
    const int jt = wv >> 1;
    const int r = lane & 31, hh = lane >> 5;
    const int grp = lane >> 4, q = (lane & 15) >> 2, pp = lane & 3;
    const int arow0 = 8 * hh + q, arow1 = arow0 + 4;
    const int ach = 4 * fi + 2 * (grp & 1) + (pp >> 1);
    const int af0 = arow0 * 256 + ((ach ^ wswz(arow0)) << 4) + 8 * (pp & 1);
    const int af1 = arow1 * 256 + ((ach ^ wswz(arow1)) << 4) + 8 * (pp & 1);
    const int ag0 = arow0 * 256 + (((ach + 8) ^ wswz(arow0)) << 4) + 8 * (pp & 1);
    const int ag1 = arow1 * 256 + (((ach + 8) ^ wswz(arow1)) << 4) + 8 * (pp & 1);

    f32x16 accF[2], accG[2];
#pragma unroll
    for (int i = 0; i < 16; ++i) { accF[0][i] = 0.f; accF[1][i] = 0.f; accG[0][i] = 0.f; accG[1][i] = 0.f; }

#pragma unroll
    for (int ch = 0; ch < 12; ++ch) {
        if (ch < 9) asm volatile("s_waitcnt vmcnt(12)" ::: "memory");
        else if (ch == 9) asm volatile("s_waitcnt vmcnt(8)" ::: "memory");
        else if (ch == 10) asm volatile("s_waitcnt vmcnt(4)" ::: "memory");
        else asm volatile("s_waitcnt vmcnt(0)" ::: "memory");
        RAW_BARRIER();
        if (ch == 0 && t0 == 0) {
            if (tid < 128) *(u32x4*)(xs + tid * 16) = (u32x4){0u, 0u, 0u, 0u};
            RAW_BARRIER();
        }
        if (ch + 4 < 12) {
            unsigned char* wn = wsb + ((ch + 4) % NRING) * WCH_BYTES;
#pragma unroll
            for (int i = 0; i < 4; ++i) GLDS(w + woff[i] + CHE(ch + 4) * 64 * 512, wn + (wv * 4 + i) * 1024);
        }
        const unsigned char* wb = wsb + (ch % NRING) * WCH_BYTES;
        const int che = CHE(ch);
        const int tap = che >> 2;
        const int xrow = jt * 64 + r + 4 - 2 * tap;
        const unsigned char* xb = xs + xrow * 512;
        const int xsw = xrow & 15;
        const int cb = (che & 3) * 8 + hh;
#pragma unroll
        for (int ks = 0; ks < 4; ++ks) {
            s16x4 f0 = trread(wb + af0 + ks * 4096);
            s16x4 f1 = trread(wb + af1 + ks * 4096);
            s16x4 g0 = trread(wb + ag0 + ks * 4096);
            s16x4 g1 = trread(wb + ag1 + ks * 4096);
            const int xo = ((cb + 2 * ks) ^ xsw) << 4;
            bf16x8 bx0 = *(const bf16x8*)(xb + xo);
            bf16x8 bx1 = *(const bf16x8*)(xb + xo + 32 * 512);
            bf16x8 af = cat8(f0, f1), ag = cat8(g0, g1);
            accF[0] = MFMA(af, bx0, accF[0]);
            accG[0] = MFMA(ag, bx0, accG[0]);
            accF[1] = MFMA(af, bx1, accF[1]);
            accG[1] = MFMA(ag, bx1, accG[1]);
        }
    }

    RAW_BARRIER();
#pragma unroll
    for (int i = 0; i < 8; ++i) {
        int pos = (wv * 8 + i) * 64 + lane;
        int row = pos >> 3, chn = (pos & 7) ^ (((row >> 1) & 1) << 2);
        GLDS(wo + row * DD + nq * 64 + chn * 8, wsb + (wv * 8 + i) * 1024);
    }
#pragma unroll
    for (int tt = 0; tt < 2; ++tt) {
        int t = t0 + jt * 64 + tt * 32 + r;
        if (t < TT) {
            unsigned short* zr = z + ((size_t)b * TT + t) * DD + nq * 64 + 32 * fi + 4 * hh;
#pragma unroll
            for (int g4 = 0; g4 < 4; ++g4) {
                u32x2 v;
                v[0] = pack2(gate(accF[tt][4 * g4 + 0], accG[tt][4 * g4 + 0]), gate(accF[tt][4 * g4 + 1], accG[tt][4 * g4 + 1]));
                v[1] = pack2(gate(accF[tt][4 * g4 + 2], accG[tt][4 * g4 + 2]), gate(accF[tt][4 * g4 + 3], accG[tt][4 * g4 + 3]));
                asm volatile("global_store_dwordx2 %0, %1, off sc0 sc1" :: "v"(zr + 8 * g4), "v"(v) : "memory");
            }
        }
    }
    asm volatile("s_waitcnt vmcnt(0)" ::: "memory");
    RAW_BARRIER();
    const unsigned long long MAGIC = 0x9E3779B97F4A7C15ull;
    if (tid < 4 && tid != nq) {
        __hip_atomic_store(flags + (mt * 4 + tid) * 4 + nq, MAGIC, __ATOMIC_RELAXED, __HIP_MEMORY_SCOPE_AGENT);
        unsigned long long* mine = flags + (mt * 4 + nq) * 4 + tid;
        while (__hip_atomic_load(mine, __ATOMIC_RELAXED, __HIP_MEMORY_SCOPE_AGENT) != MAGIC) __builtin_amdgcn_s_sleep(1);
        __hip_atomic_store(mine, 0ull, __ATOMIC_RELAXED, __HIP_MEMORY_SCOPE_AGENT);
    }
    RAW_BARRIER();

    int t2 = t0 + wv * 32 + r;
    const bool tv = t2 < TT;
    int t2c = tv ? t2 : t0;
    const unsigned short* zr2 = z + ((size_t)b * TT + t2c) * DD + hh * 8;
    bf16x8 zf[16];
#pragma unroll
    for (int ks = 0; ks < 16; ++ks)
        asm volatile("global_load_dwordx4 %0, %1, off sc0 sc1" : "=v"(zf[ks]) : "v"(zr2 + ks * 16) : "memory");
    asm volatile("s_waitcnt vmcnt(0)" ::: "memory");
    RAW_BARRIER();
    const int sw0 = ((arow0 >> 1) & 1) << 2, sw1 = ((arow1 >> 1) & 1) << 2;
    const int oc = 2 * (grp & 1) + (pp >> 1);
    const unsigned char* p00 = wsb + arow0 * 128 + ((oc ^ sw0) << 4) + 8 * (pp & 1);
    const unsigned char* p01 = wsb + arow1 * 128 + ((oc ^ sw1) << 4) + 8 * (pp & 1);
    const unsigned char* p10 = wsb + arow0 * 128 + (((oc + 4) ^ sw0) << 4) + 8 * (pp & 1);
    const unsigned char* p11 = wsb + arow1 * 128 + (((oc + 4) ^ sw1) << 4) + 8 * (pp & 1);
    f32x16 acc[2];
#pragma unroll
    for (int i = 0; i < 16; ++i) { acc[0][i] = 0.f; acc[1][i] = 0.f; }
#pragma unroll
    for (int ks = 0; ks < 16; ++ks) {
        acc[0] = MFMA(cat8(trread(p00 + ks * 2048), trread(p01 + ks * 2048)), zf[ks], acc[0]);
        acc[1] = MFMA(cat8(trread(p10 + ks * 2048), trread(p11 + ks * 2048)), zf[ks], acc[1]);
    }
    if (tv) {
        unsigned short* orow = out + ((size_t)b * TT + t2) * DD + nq * 64 + 4 * hh;
#pragma unroll
        for (int ot = 0; ot < 2; ++ot)
#pragma unroll
            for (int g4 = 0; g4 < 4; ++g4) {
                u32x2 v;
                v[0] = pack2(acc[ot][4 * g4 + 0], acc[ot][4 * g4 + 1]);
                v[1] = pack2(acc[ot][4 * g4 + 2], acc[ot][4 * g4 + 3]);
                *(u32x2*)(orow + ot * 32 + 8 * g4) = v;
            }
    }
}

extern "C" void kernel_launch(void* const* d_in, const int* in_sizes, int n_in,
                              void* d_out, int out_size, void* d_ws, size_t ws_size,
                              hipStream_t stream) {
    const unsigned short* x = (const unsigned short*)d_in[0];
    const unsigned short* w = (const unsigned short*)d_in[1];
    const unsigned short* wo = (const unsigned short*)d_in[2];
    unsigned short* out = (unsigned short*)d_out;
    unsigned short* z = (unsigned short*)d_ws;
    unsigned long long* flags = (unsigned long long*)((char*)d_ws + (8 << 20));
    k1_conv_gate<<<256, 256, 0, stream>>>(x, w, wo, z, flags, out);
}
```
